```python
import torch

def _softmax(x, axis=-1):
    return torch.softmax(x, dim=axis)
def _gelu(x):
    return torch.nn.functional.gelu(x, approximate="tanh")
def _silu(x):
    return torch.nn.functional.silu(x)
def _sigmoid(x):
    return torch.sigmoid(x)
def _layernorm(x, axis=-1, eps=1e-5):
    m = x.mean(dim=axis, keepdim=True)
    v = x.var(dim=axis, keepdim=True, unbiased=False)
    return (x - m) / torch.sqrt(v + eps)
def _scatter_add(tgt, idx, src, axis=0):
    return tgt.clone().index_add_(axis, idx, src)
def _segment_sum(src, seg, n):
    out = torch.zeros((n,) + src.shape[1:], dtype=src.dtype)
    return out.index_add_(0, seg, src)
def _pad(x, pw):
    flat = tuple(v for pair in reversed(pw) for v in pair)
    return torch.nn.functional.pad(x, flat)

kernel_name = 'concat_heads__gqa_block_cached__gqa_block_cached__near_ridge'

def setup_inputs():

    _w = (lambda a: (lambda t: t.to(torch.bfloat16) if t.is_floating_point() else t)(torch.as_tensor(a)))
    def _A_setup():
        B, Sq, D = 8, 96, 448
        Hq, Hkv, Sk, Dh = 16, 8, 1024, 256
        assert Hq % Hkv == 0, (Hq, Hkv)
        return {
            "x": _w((lambda *s: torch.randn(*s, dtype=torch.bfloat16) * 0.8)(B, Sq, D)),
            "k_cache": _w((lambda *s: torch.randn(*s, dtype=torch.bfloat16) * 0.8)(B, Hkv, Sk, Dh)),
            "v_cache": _w((lambda *s: torch.randn(*s, dtype=torch.bfloat16) * 0.05)(B, Hkv, Sk, Dh)),
            "wq": _w((lambda *s: torch.randn(*s, dtype=torch.bfloat16) * 0.8)(D, Hq * Dh) * (D ** -0.5)),
            "wo": _w((lambda *s: torch.randn(*s, dtype=torch.bfloat16) * 0.05)(Hq * Dh, D) * ((Hq * Dh) ** -0.5)),
        }
    _A_in = _A_setup()

    def _B_setup():
        B, Sq, D = 8, 96, 448
        Hq, Hkv, Sk, Dh = 16, 8, 1024, 256
        assert Hq % Hkv == 0, (Hq, Hkv)
        return {
            "x": _w((lambda *s: torch.randn(*s, dtype=torch.bfloat16) * 0.8)(B, Sq, D)),
            "k_cache": _w((lambda *s: torch.randn(*s, dtype=torch.bfloat16) * 0.8)(B, Hkv, Sk, Dh)),
            "v_cache": _w((lambda *s: torch.randn(*s, dtype=torch.bfloat16) * 0.05)(B, Hkv, Sk, Dh)),
            "wq": _w((lambda *s: torch.randn(*s, dtype=torch.bfloat16) * 0.8)(D, Hq * Dh) * (D ** -0.5)),
            "wo": _w((lambda *s: torch.randn(*s, dtype=torch.bfloat16) * 0.05)(Hq * Dh, D) * ((Hq * Dh) ** -0.5)),
        }
    _B_in = _B_setup()

    return {
        "A_x": _A_in["x"],
        "A_k_cache": _A_in["k_cache"],
        "A_v_cache": _A_in["v_cache"],
        "A_wq": _A_in["wq"],
        "A_wo": _A_in["wo"],
        "B_x": _B_in["x"],
        "B_k_cache": _B_in["k_cache"],
        "B_v_cache": _B_in["v_cache"],
        "B_wq": _B_in["wq"],
        "B_wo": _B_in["wo"],
    }

def reference(A_x, A_k_cache, A_v_cache, A_wq, A_wo, B_x, B_k_cache, B_v_cache, B_wq, B_wo):
    def _A_ref(x, k_cache, v_cache, wq, wo):
        B, Sq, D = x.shape
        Hq, Dh = 16, 256
        Hkv = k_cache.shape[1]
        rep = Hq // Hkv
        q = (lambda x, *d: x.permute(*d))((x @ wq).reshape(B, Sq, Hq, Dh), 0, 2, 1, 3)
        k = (lambda x, n, axis: torch.repeat_interleave(x, n, dim=axis))(k_cache, rep, axis=1)
        v = (lambda x, n, axis: torch.repeat_interleave(x, n, dim=axis))(v_cache, rep, axis=1)
        scores = torch.einsum("bhqd,bhkd->bhqk", q, k) * (Dh ** -0.5)
        p = _softmax(scores, axis=-1)
        o = torch.einsum("bhqk,bhkd->bhqd", p, v)
        return (lambda x, *d: x.permute(*d))(o, 0, 2, 1, 3).reshape(B, Sq, Hq * Dh) @ wo

    def _B_ref(x, k_cache, v_cache, wq, wo):
        B, Sq, D = x.shape
        Hq, Dh = 16, 256
        Hkv = k_cache.shape[1]
        rep = Hq // Hkv
        q = (lambda x, *d: x.permute(*d))((x @ wq).reshape(B, Sq, Hq, Dh), 0, 2, 1, 3)
        k = (lambda x, n, axis: torch.repeat_interleave(x, n, dim=axis))(k_cache, rep, axis=1)
        v = (lambda x, n, axis: torch.repeat_interleave(x, n, dim=axis))(v_cache, rep, axis=1)
        scores = torch.einsum("bhqd,bhkd->bhqk", q, k) * (Dh ** -0.5)
        p = _softmax(scores, axis=-1)
        o = torch.einsum("bhqk,bhkd->bhqd", p, v)
        return (lambda x, *d: x.permute(*d))(o, 0, 2, 1, 3).reshape(B, Sq, Hq * Dh) @ wo

    a = _A_ref(A_x, A_k_cache, A_v_cache, A_wq, A_wo)
    b = _B_ref(B_x, B_k_cache, B_v_cache, B_wq, B_wo)
    return torch.concatenate([a, b], axis=-1)
```

```cpp
#include <hip/hip_runtime.h>
#include <hip/hip_bf16.h>
#include <stdint.h>
#include <type_traits>

typedef __attribute__((ext_vector_type(8))) short bf16x8;
typedef __attribute__((ext_vector_type(4))) short s16x4;
typedef __attribute__((ext_vector_type(4))) float f32x4;
typedef __attribute__((ext_vector_type(2))) float f32x2;
typedef __attribute__((ext_vector_type(2))) __bf16 bf16x2_t;
typedef __attribute__((ext_vector_type(4))) unsigned u32x4;
typedef __attribute__((ext_vector_type(2))) unsigned u32x2;
typedef __attribute__((address_space(3))) s16x4 lds_s16x4;

constexpr int NBATCH = 8, SQ = 96, DM = 448, HQ = 16, HKV = 8, SK = 1024, DH = 256;
constexpr int MROWS = NBATCH * SQ;
constexpr int HD = HQ * DH;
constexpr float SCALE_LOG2E = 0.0625f * 1.4426950408889634f;

__device__ __forceinline__ unsigned pack_bf16(float lo, float hi) {
  f32x2 v = {lo, hi};
  bf16x2_t b = __builtin_convertvector(v, bf16x2_t);
  return __builtin_bit_cast(unsigned, b);
}

__device__ __forceinline__ s16x4 tr_read(const char* lds_ptr) {
  return __builtin_amdgcn_ds_read_tr16_b64_v4i16((lds_s16x4*)(lds_ptr));
}

template <int BM>
__global__ __launch_bounds__(256) void gemm_kernel(const short* __restrict__ A0, const short* __restrict__ A1,
                                                   const short* __restrict__ B0, const short* __restrict__ B1,
                                                   short* __restrict__ C0, short* __restrict__ C1,
                                                   int lda, int ldb, int ldc, int K, int ntn, int mode) {
  constexpr int BN = 64, BK = 64;
  constexpr int WM = BM / 48;
  constexpr int WN = 4 / WM;
  constexpr int NT = BN / WN / 16;
  __shared__ __attribute__((aligned(16))) char smem[2 * (BM * BK * 2 + BK * BN * 2)];
  const int tid = threadIdx.x, lane = tid & 63, w = tid >> 6;
  const int r = lane & 15, g = lane >> 4;
  const int blk = blockIdx.y;
  const int tm = blockIdx.x / ntn, tn = blockIdx.x % ntn;
  const short* A = blk ? A1 : A0;
  const short* B = blk ? B1 : B0;
  short* C = blk ? C1 : C0;
  const int m0 = tm * BM, n0 = tn * BN;
  const int wm = w % WM, wn = w / WM;

  f32x4 acc[3][NT];
#pragma unroll
  for (int i = 0; i < 3; ++i)
#pragma unroll
    for (int j = 0; j < NT; ++j) acc[i][j] = f32x4{0.f, 0.f, 0.f, 0.f};

  constexpr int ACH = BM * 8;
  constexpr int AIT = (ACH + 255) / 256;
  bf16x8 ra[AIT], rb[2];
  auto gload = [&](int k0) {
#pragma unroll
    for (int i = 0; i < AIT; ++i) {
      int c = tid + i * 256;
      if (c < ACH) ra[i] = *(const bf16x8*)(A + (size_t)(m0 + (c >> 3)) * lda + k0 + (c & 7) * 8);
    }
#pragma unroll
    for (int i = 0; i < 2; ++i) {
      int c = tid + i * 256;
      rb[i] = *(const bf16x8*)(B + (size_t)(k0 + (c >> 3)) * ldb + n0 + (c & 7) * 8);
    }
  };
  auto swrite = [&](int buf) {
    char* sa = smem + buf * (BM * BK * 2 + BK * BN * 2);
    char* sb = sa + BM * BK * 2;
#pragma unroll
    for (int i = 0; i < AIT; ++i) {
      int c = tid + i * 256;
      if (c < ACH) {
        int row = c >> 3, ch = c & 7;
        *(bf16x8*)(sa + row * 128 + ((ch ^ (row & 7)) * 16)) = ra[i];
      }
    }
#pragma unroll
    for (int i = 0; i < 2; ++i) {
      int c = tid + i * 256;
      int row = c >> 3, ch = c & 7;
      int f = ((row >> 1) & 1) | (((row >> 3) & 1) << 1);
      *(bf16x8*)(sb + row * 128 + ((ch ^ (f << 1)) * 16)) = rb[i];
    }
  };
  const int nk = K / BK;
  gload(0);
  swrite(0);
  __syncthreads();
  for (int kt = 0; kt < nk; ++kt) {
    const int buf = kt & 1;
    if (kt + 1 < nk) gload((kt + 1) * BK);
    const char* sa = smem + buf * (BM * BK * 2 + BK * BN * 2);
    const char* sb = sa + BM * BK * 2;
#pragma unroll
    for (int ks = 0; ks < 2; ++ks) {
      bf16x8 af[3], bfr[NT];
#pragma unroll
      for (int i = 0; i < 3; ++i) {
        int row = wm * 48 + i * 16 + r;
        af[i] = *(const bf16x8*)(sa + row * 128 + (((ks * 4 + g) ^ (row & 7)) * 16));
      }
#pragma unroll
      for (int j = 0; j < NT; ++j) {
        int ncol = wn * (NT * 16) + j * 16;
        s16x4 lo, hi;
        {
          int row = ks * 32 + 8 * g + (r >> 2);
          int col = ncol + (r & 3) * 4;
          int f = ((row >> 1) & 1) | (((row >> 3) & 1) << 1);
          lo = tr_read(sb + row * 128 + (((col >> 3) ^ (f << 1)) * 16) + (col & 7) * 2);
          row += 4;
          f = ((row >> 1) & 1) | (((row >> 3) & 1) << 1);
          hi = tr_read(sb + row * 128 + (((col >> 3) ^ (f << 1)) * 16) + (col & 7) * 2);
        }
        bfr[j] = bf16x8{lo[0], lo[1], lo[2], lo[3], hi[0], hi[1], hi[2], hi[3]};
      }
#pragma unroll
      for (int i = 0; i < 3; ++i)
#pragma unroll
        for (int j = 0; j < NT; ++j)
          acc[i][j] = __builtin_amdgcn_mfma_f32_16x16x32_bf16(af[i], bfr[j], acc[i][j], 0, 0, 0);
    }
    if (kt + 1 < nk) swrite(buf ^ 1);
    __syncthreads();
  }
#pragma unroll
  for (int i = 0; i < 3; ++i)
#pragma unroll
    for (int j = 0; j < NT; ++j)
#pragma unroll
      for (int e = 0; e < 4; ++e) {
        int m = m0 + wm * 48 + i * 16 + 4 * g + e;
        int n = n0 + wn * (NT * 16) + j * 16 + r;
        __hip_bfloat16 v = __float2bfloat16(acc[i][j][e]);
        size_t idx;
        if (mode == 0) {
          int b = m / SQ, s = m % SQ, h = n / DH, d = n % DH;
          idx = (((size_t)b * HQ + h) * SQ + s) * DH + d;
        } else {
          idx = (size_t)m * ldc + n;
        }
        C[idx] = *(short*)&v;
      }
}

#define OCLOB "a0","a1","a2","a3","a4","a5","a6","a7","a8","a9","a10","a11","a12","a13","a14","a15","a16","a17","a18","a19","a20","a21","a22","a23","a24","a25","a26","a27","a28","a29","a30","a31","a32","a33","a34","a35","a36","a37","a38","a39","a40","a41","a42","a43","a44","a45","a46","a47","a48","a49","a50","a51","a52","a53","a54","a55","a56","a57","a58","a59","a60","a61","a62","a63","a64","a65","a66","a67","a68","a69","a70","a71","a72","a73","a74","a75","a76","a77","a78","a79","a80","a81","a82","a83","a84","a85","a86","a87","a88","a89","a90","a91","a92","a93","a94","a95","a96","a97","a98","a99","a100","a101","a102","a103","a104","a105","a106","a107","a108","a109","a110","a111","a112","a113","a114","a115","a116","a117","a118","a119","a120","a121","a122","a123","a124","a125","a126","a127","a128","a129","a130","a131","a132","a133","a134","a135","a136","a137","a138","a139","a140","a141","a142","a143","a144","a145","a146","a147","a148","a149","a150","a151","a152","a153","a154","a155","a156","a157","a158","a159","a160","a161","a162","a163","a164","a165","a166","a167","a168","a169","a170","a171","a172","a173","a174","a175","a176","a177","a178","a179","a180","a181","a182","a183","a184","a185","a186","a187","a188","a189","a190","a191"
template <int IDX> __device__ __forceinline__ void o_mfma(bf16x8 vf, bf16x8 pf) {
  asm volatile("v_mfma_f32_16x16x32_bf16 a[%c2:%c3], %0, %1, a[%c2:%c3]" :: "v"(vf), "v"(pf), "i"(IDX * 4), "i"(IDX * 4 + 3) : OCLOB);
}
template <int IDX> __device__ __forceinline__ void o_zero() {
  asm volatile("v_accvgpr_write_b32 a[%c0], 0\n\tv_accvgpr_write_b32 a[%c1], 0\n\tv_accvgpr_write_b32 a[%c2], 0\n\tv_accvgpr_write_b32 a[%c3], 0"
               :: "i"(IDX * 4), "i"(IDX * 4 + 1), "i"(IDX * 4 + 2), "i"(IDX * 4 + 3) : OCLOB);
}
template <int IDX> __device__ __forceinline__ void o_scale(float al) {
  float t0, t1, t2, t3;
  asm volatile("v_accvgpr_read_b32 %0, a[%c5]\n\tv_accvgpr_read_b32 %1, a[%c6]\n\tv_accvgpr_read_b32 %2, a[%c7]\n\tv_accvgpr_read_b32 %3, a[%c8]\n\t"
               "v_mul_f32 %0, %0, %4\n\tv_mul_f32 %1, %1, %4\n\tv_mul_f32 %2, %2, %4\n\tv_mul_f32 %3, %3, %4\n\t"
               "v_accvgpr_write_b32 a[%c5], %0\n\tv_accvgpr_write_b32 a[%c6], %1\n\tv_accvgpr_write_b32 a[%c7], %2\n\tv_accvgpr_write_b32 a[%c8], %3"
               : "=&v"(t0), "=&v"(t1), "=&v"(t2), "=&v"(t3)
               : "v"(al), "i"(IDX * 4), "i"(IDX * 4 + 1), "i"(IDX * 4 + 2), "i"(IDX * 4 + 3) : OCLOB);
}
template <int IDX> __device__ __forceinline__ f32x4 o_read() {
  float t0, t1, t2, t3;
  asm volatile("v_accvgpr_read_b32 %0, a[%c4]\n\tv_accvgpr_read_b32 %1, a[%c5]\n\tv_accvgpr_read_b32 %2, a[%c6]\n\tv_accvgpr_read_b32 %3, a[%c7]"
               : "=v"(t0), "=v"(t1), "=v"(t2), "=v"(t3)
               : "i"(IDX * 4), "i"(IDX * 4 + 1), "i"(IDX * 4 + 2), "i"(IDX * 4 + 3) : OCLOB);
  return f32x4{t0, t1, t2, t3};
}
__device__ __forceinline__ void s_mfma0(f32x4& d, bf16x8 a, bf16x8 b) {
  asm volatile("v_mfma_f32_16x16x32_bf16 %0, %1, %2, 0" : "=&v"(d) : "v"(a), "v"(b));
}
__device__ __forceinline__ void s_mfma(f32x4& d, bf16x8 a, bf16x8 b) {
  asm volatile("v_mfma_f32_16x16x32_bf16 %0, %1, %2, %0" : "+v"(d) : "v"(a), "v"(b));
}
template <int N, typename F> __device__ __forceinline__ void static_for(F&& f) {
  if constexpr (N > 0) { static_for<N - 1>(f); f(std::integral_constant<int, N - 1>{}); }
}

constexpr int KT = 64;
constexpr int TILE_B = KT * DH * 2;
constexpr int ATT_LDS = 4 * TILE_B;

__global__ __launch_bounds__(256, 1) void attn_kernel(const short* __restrict__ q_ws,
                                                      const short* __restrict__ K0, const short* __restrict__ V0,
                                                      const short* __restrict__ K1, const short* __restrict__ V1,
                                                      short* __restrict__ o_ws) {
  extern __shared__ __attribute__((aligned(16))) char smem[];
  const int tid = threadIdx.x, lane = tid & 63;
  const int w = __builtin_amdgcn_readfirstlane(tid >> 6);
  const int r = lane & 15, g = lane >> 4;
  const int rh = w & 1, kh = w >> 1;
  const int id = blockIdx.x;
  const int xcd = id & 7, slot = id >> 3;
  const int pair = xcd * 16 + (slot >> 1);
  const int hq = (pair & 7) * 2 + (slot & 1);
  const int b = (pair >> 3) & 7, blk = pair >> 6;
  const int hkv = hq >> 1;
  const short* Kg = (blk ? K1 : K0) + ((size_t)b * HKV + hkv) * SK * DH;
  const short* Vg = (blk ? V1 : V0) + ((size_t)b * HKV + hkv) * SK * DH;
  const short* Qg = q_ws + (((size_t)blk * NBATCH + b) * HQ + hq) * SQ * DH;

  bf16x8 qf[3][8];
#pragma unroll
  for (int rt = 0; rt < 3; ++rt)
#pragma unroll
    for (int s = 0; s < 8; ++s)
      qf[rt][s] = *(const bf16x8*)(Qg + (size_t)(rh * 48 + rt * 16 + r) * DH + s * 32 + g * 8);

  static_for<48>([&](auto I) { o_zero<I.value>(); });
  float mrow[3] = {-INFINITY, -INFINITY, -INFINITY};
  float lrow[3] = {0.f, 0.f, 0.f};

  auto stage = [&](int t, int buf) {
    char* kd = smem + buf * TILE_B;
    char* vd = smem + (2 + buf) * TILE_B;
    int ln = lane;
    asm volatile("" : "+v"(ln));
    const int hh = ln >> 5, cp = ln & 31;
    const short* kbase = Kg + (size_t)(t * KT + w * 16 + hh) * DH;
    const short* vbase = Vg + (size_t)(t * KT + w * 16 + hh) * DH;
#pragma unroll
    for (int i = 0; i < 8; ++i) {
      int inst = w * 8 + i;
      int k15 = (2 * i + hh) & 15;
      const short* ks = kbase + i * 2 * DH + ((cp ^ k15) * 8);
      __builtin_amdgcn_global_load_lds((const unsigned*)ks, (unsigned*)(kd + inst * 1024), 16, 0, 0);
      const short* vs = vbase + i * 2 * DH + ((cp ^ ((k15 & 7) << 1)) * 8);
      __builtin_amdgcn_global_load_lds((const unsigned*)vs, (unsigned*)(vd + inst * 1024), 16, 0, 0);
    }
  };

  int kaddr[4], vaddr[8];
#pragma unroll
  for (int c = 0; c < 4; ++c) kaddr[c] = (kh * 32 + r) * 512 + (((4 * c + g) ^ r) * 16);
  {
    int q = r >> 2, p4 = r & 3;
    int key0 = kh * 32 + 4 * g + q;
#pragma unroll
    for (int c = 0; c < 8; ++c)
      vaddr[c] = 2 * TILE_B + key0 * 512 + (((2 * c + (p4 >> 1)) ^ ((key0 & 7) << 1)) * 16) + 8 * (p4 & 1);
  }

  constexpr int NTILE = SK / KT;
  stage(0, 0);
  asm volatile("s_waitcnt vmcnt(0)" ::: "memory");
  __syncthreads();
#pragma unroll 2
  for (int t = 0; t < NTILE; ++t) {
    const int buf = t & 1;
    if (t + 1 < NTILE) stage(t + 1, buf ^ 1);
    f32x4 st[3][2];
#pragma unroll
    for (int s = 0; s < 8; ++s) {
#pragma unroll
      for (int kt = 0; kt < 2; ++kt) {
        bf16x8 kf = *(const bf16x8*)(smem + kaddr[s & 3] + buf * TILE_B + (s >> 2) * 256 + kt * 8192);
#pragma unroll
        for (int rt = 0; rt < 3; ++rt)
        {
          if (s == 0) s_mfma0(st[rt][kt], kf, qf[rt][s]);
          else s_mfma(st[rt][kt], kf, qf[rt][s]);
        }
      }
    }
    asm volatile("s_nop 7\n\ts_nop 7" : "+v"(st[0][0]), "+v"(st[0][1]), "+v"(st[1][0]), "+v"(st[1][1]), "+v"(st[2][0]), "+v"(st[2][1]));
    bf16x8 pf[3];
    float al[3];
#pragma unroll
    for (int rt = 0; rt < 3; ++rt) {
      float mx = fmaxf(fmaxf(fmaxf(st[rt][0][0], st[rt][0][1]), fmaxf(st[rt][0][2], st[rt][0][3])),
                       fmaxf(fmaxf(st[rt][1][0], st[rt][1][1]), fmaxf(st[rt][1][2], st[rt][1][3])));
      mx = fmaxf(mx, __shfl_xor(mx, 16));
      mx = fmaxf(mx, __shfl_xor(mx, 32));
      mx *= SCALE_LOG2E;
      float mnew = fmaxf(mrow[rt], mx);
      float alpha = __builtin_amdgcn_exp2f(mrow[rt] - mnew);
      mrow[rt] = mnew;
      float p[8];
#pragma unroll
      for (int kt = 0; kt < 2; ++kt)
#pragma unroll
        for (int i = 0; i < 4; ++i) p[kt * 4 + i] = __builtin_amdgcn_exp2f(fmaf(st[rt][kt][i], SCALE_LOG2E, -mnew));
      float ps = ((p[0] + p[1]) + (p[2] + p[3])) + ((p[4] + p[5]) + (p[6] + p[7]));
      lrow[rt] = lrow[rt] * alpha + ps;
      u32x4 pw = {pack_bf16(p[0], p[1]), pack_bf16(p[2], p[3]), pack_bf16(p[4], p[5]), pack_bf16(p[6], p[7])};
      pf[rt] = __builtin_bit_cast(bf16x8, pw);
      al[rt] = alpha;
    }
    if (__any((al[0] != 1.f) | (al[1] != 1.f) | (al[2] != 1.f))) {
      static_for<48>([&](auto I) { o_scale<I.value>(al[I.value / 16]); });
    }
    asm volatile("s_nop 4" : "+v"(pf[0]), "+v"(pf[1]), "+v"(pf[2]));
    static_for<16>([&](auto D) {
      constexpr int dt = D.value;
      const char* va = smem + vaddr[dt & 7] + buf * TILE_B + (dt >> 3) * 256;
      s16x4 lo = tr_read(va);
      s16x4 hi = tr_read(va + 8192);
      bf16x8 vf = bf16x8{lo[0], lo[1], lo[2], lo[3], hi[0], hi[1], hi[2], hi[3]};
      o_mfma<dt>(vf, pf[0]);
      o_mfma<16 + dt>(vf, pf[1]);
      o_mfma<32 + dt>(vf, pf[2]);
    });
    asm volatile("s_waitcnt vmcnt(0)" ::: "memory");
    __syncthreads();
  }
#pragma unroll
  for (int rt = 0; rt < 3; ++rt) {
    lrow[rt] += __shfl_xor(lrow[rt], 16);
    lrow[rt] += __shfl_xor(lrow[rt], 32);
  }
  float* xo = (float*)smem;
  float* xml = (float*)(smem + 4 * 3 * 8 * 64 * 16);
  const int khu = __builtin_amdgcn_readfirstlane(kh);
  {
    float* dst = xo + (size_t)w * 3 * 8 * 256;
    asm volatile("s_nop 15\n\ts_nop 15");
    if (khu) {
      static_for<24>([&](auto I) { constexpr int rt = I.value / 8, j = I.value % 8;
        *(f32x4*)(dst + (rt * 8 + j) * 256 + lane * 4) = o_read<rt * 16 + j>(); });
    } else {
      static_for<24>([&](auto I) { constexpr int rt = I.value / 8, j = I.value % 8;
        *(f32x4*)(dst + (rt * 8 + j) * 256 + lane * 4) = o_read<rt * 16 + 8 + j>(); });
    }
    if (g == 0) {
#pragma unroll
      for (int rt = 0; rt < 3; ++rt) {
        xml[((w * 3 + rt) * 16 + r) * 2 + 0] = mrow[rt];
        xml[((w * 3 + rt) * 16 + r) * 2 + 1] = lrow[rt];
      }
    }
  }
  __syncthreads();
  {
    const int pw = w ^ 2;
    const float* src = xo + (size_t)pw * 3 * 8 * 256;
    float a1[3], a2[3];
#pragma unroll
    for (int rt = 0; rt < 3; ++rt) {
      float m2 = xml[((pw * 3 + rt) * 16 + r) * 2 + 0];
      float l2 = xml[((pw * 3 + rt) * 16 + r) * 2 + 1];
      float mm = fmaxf(mrow[rt], m2);
      a1[rt] = __builtin_amdgcn_exp2f(mrow[rt] - mm);
      a2[rt] = __builtin_amdgcn_exp2f(m2 - mm);
      float inv = 1.0f / (lrow[rt] * a1[rt] + l2 * a2[rt]);
      a1[rt] *= inv; a2[rt] *= inv;
    }
    auto fin = [&](int rt, int j, f32x4 mine) {
      int row = b * SQ + rh * 48 + rt * 16 + r;
      short* dst = o_ws + ((size_t)blk * MROWS + row) * HD + hq * DH + khu * 128;
      f32x4 other = *(const f32x4*)(src + (rt * 8 + j) * 256 + lane * 4);
      u32x2 pk = {pack_bf16(mine[0] * a1[rt] + other[0] * a2[rt], mine[1] * a1[rt] + other[1] * a2[rt]),
                  pack_bf16(mine[2] * a1[rt] + other[2] * a2[rt], mine[3] * a1[rt] + other[3] * a2[rt])};
      *(u32x2*)(dst + j * 16 + 4 * g) = pk;
    };
    if (khu) {
      static_for<24>([&](auto I) { constexpr int rt = I.value / 8, j = I.value % 8; fin(rt, j, o_read<rt * 16 + 8 + j>()); });
    } else {
      static_for<24>([&](auto I) { constexpr int rt = I.value / 8, j = I.value % 8; fin(rt, j, o_read<rt * 16 + j>()); });
    }
  }
}

extern "C" void kernel_launch(void* const* d_in, const int* in_sizes, int n_in,
                              void* d_out, int out_size, void* d_ws, size_t ws_size,
                              hipStream_t stream) {
  const short* Ax = (const short*)d_in[0];
  const short* Ak = (const short*)d_in[1];
  const short* Av = (const short*)d_in[2];
  const short* Awq = (const short*)d_in[3];
  const short* Awo = (const short*)d_in[4];
  const short* Bx = (const short*)d_in[5];
  const short* Bk = (const short*)d_in[6];
  const short* Bv = (const short*)d_in[7];
  const short* Bwq = (const short*)d_in[8];
  const short* Bwo = (const short*)d_in[9];
  short* out = (short*)d_out;
  short* q_ws = (short*)d_ws;
  short* o_ws = q_ws + (size_t)2 * MROWS * HD;

  hipFuncSetAttribute((const void*)attn_kernel, hipFuncAttributeMaxDynamicSharedMemorySize, ATT_LDS);

  gemm_kernel<96><<<dim3(8 * 64, 2), 256, 0, stream>>>(Ax, Bx, Awq, Bwq, q_ws, q_ws + (size_t)MROWS * HD,
                                                       DM, HD, 0, DM, 64, 0);
  attn_kernel<<<256, 256, ATT_LDS, stream>>>(q_ws, Ak, Av, Bk, Bv, o_ws);
  gemm_kernel<48><<<dim3(16 * 7, 2), 256, 0, stream>>>(o_ws, o_ws + (size_t)MROWS * HD, Awo, Bwo, out, out + DM,
                                                       HD, DM, 2 * DM, HD, 7, 1);
}
```

```cpp
#include <hip/hip_runtime.h>
#include <hip/hip_bf16.h>
#include <stdint.h>
#include <type_traits>

typedef __attribute__((ext_vector_type(8))) short bf16x8;
typedef __attribute__((ext_vector_type(4))) short s16x4;
typedef __attribute__((ext_vector_type(4))) float f32x4;
typedef __attribute__((ext_vector_type(2))) float f32x2;
typedef __attribute__((ext_vector_type(2))) __bf16 bf16x2_t;
typedef __attribute__((ext_vector_type(4))) unsigned u32x4;
typedef __attribute__((ext_vector_type(2))) unsigned u32x2;
typedef __attribute__((address_space(3))) s16x4 lds_s16x4;

constexpr int NBATCH = 8, SQ = 96, DM = 448, HQ = 16, HKV = 8, SK = 1024, DH = 256;
constexpr int MROWS = NBATCH * SQ;
constexpr int HD = HQ * DH;
constexpr float SCALE_LOG2E = 0.0625f * 1.4426950408889634f;

__device__ __forceinline__ unsigned pack_bf16(float lo, float hi) {
  f32x2 v = {lo, hi};
  bf16x2_t b = __builtin_convertvector(v, bf16x2_t);
  return __builtin_bit_cast(unsigned, b);
}

__device__ __forceinline__ s16x4 tr_read(const char* lds_ptr) {
  return __builtin_amdgcn_ds_read_tr16_b64_v4i16((lds_s16x4*)(lds_ptr));
}

constexpr int QB_STRIDE = 528;
constexpr int QA_BYTES = 96 * 128, QB_BYTES = 64 * QB_STRIDE, QSTAGE = QA_BYTES + QB_BYTES;
__global__ __launch_bounds__(256, 1) void qproj_kernel(const short* __restrict__ X0, const short* __restrict__ X1,
                                                       const short* __restrict__ W0, const short* __restrict__ W1,
                                                       short* __restrict__ q_ws) {
  extern __shared__ __attribute__((aligned(16))) char smem[];
  const int tid = threadIdx.x, lane = tid & 63, w = tid >> 6;
  const int r = lane & 15, g = lane >> 4;
  const int id = blockIdx.x;
  const int hq = (id & 7) | (((id >> 3) & 1) << 3), b = (id >> 4) & 7, blk = id >> 7;
  const int wm = w & 1, wn = w >> 1;
  const short* A = (blk ? X1 : X0) + (size_t)b * SQ * DM;
  const short* B = (blk ? W1 : W0) + hq * DH;

  f32x4 acc[3][8];
#pragma unroll
  for (int i = 0; i < 3; ++i)
#pragma unroll
    for (int j = 0; j < 8; ++j) acc[i][j] = f32x4{0.f, 0.f, 0.f, 0.f};

  bf16x8 ra[3][3], rb[3][8];
  auto gload = [&](int kt, int set) {
#pragma unroll
    for (int i = 0; i < 3; ++i) {
      int c = tid + i * 256;
      ra[set][i] = *(const bf16x8*)(A + (size_t)(c >> 3) * DM + kt * 64 + (c & 7) * 8);
    }
#pragma unroll
    for (int i = 0; i < 8; ++i) {
      int c = tid + i * 256;
      rb[set][i] = *(const bf16x8*)(B + (size_t)(kt * 64 + (c >> 5)) * HD + (c & 31) * 8);
    }
  };
  gload(0, 0);
  gload(1, 1);
  gload(2, 2);
#pragma unroll
  for (int kt = 0; kt < 7; ++kt) {
    const int set = kt % 3;
    char* sa = smem + (kt & 1) * QSTAGE;
    char* sb = sa + QA_BYTES;
#pragma unroll
    for (int i = 0; i < 3; ++i) {
      int c = tid + i * 256;
      int row = c >> 3, ch = c & 7;
      *(bf16x8*)(sa + row * 128 + ((ch ^ (row & 7)) * 16)) = ra[set][i];
    }
#pragma unroll
    for (int i = 0; i < 8; ++i) {
      int c = tid + i * 256;
      *(bf16x8*)(sb + (c >> 5) * QB_STRIDE + (c & 31) * 16) = rb[set][i];
    }
    __builtin_amdgcn_sched_barrier(0);
    if (kt + 3 < 7) gload(kt + 3, set);
    __builtin_amdgcn_sched_barrier(0);
    __syncthreads();
#pragma unroll
    for (int ks = 0; ks < 2; ++ks) {
      bf16x8 af[3], bfr[8];
#pragma unroll
      for (int i = 0; i < 3; ++i) {
        int row = wm * 48 + i * 16 + r;
        af[i] = *(const bf16x8*)(sa + row * 128 + (((ks * 4 + g) ^ (row & 7)) * 16));
      }
      const char* bb = sb + (ks * 32 + 8 * g + (r >> 2)) * QB_STRIDE + (wn * 128 + (r & 3) * 4) * 2;
#pragma unroll
      for (int j = 0; j < 8; ++j) {
        s16x4 lo = tr_read(bb + j * 32);
        s16x4 hi = tr_read(bb + j * 32 + 4 * QB_STRIDE);
        bfr[j] = bf16x8{lo[0], lo[1], lo[2], lo[3], hi[0], hi[1], hi[2], hi[3]};
      }
#pragma unroll
      for (int i = 0; i < 3; ++i)
#pragma unroll
        for (int j = 0; j < 8; ++j)
          acc[i][j] = __builtin_amdgcn_mfma_f32_16x16x32_bf16(bfr[j], af[i], acc[i][j], 0, 0, 0);
    }
  }
  __syncthreads();
#pragma unroll
  for (int i = 0; i < 3; ++i)
#pragma unroll
    for (int j = 0; j < 8; ++j) {
      u32x2 pk = {pack_bf16(acc[i][j][0], acc[i][j][1]), pack_bf16(acc[i][j][2], acc[i][j][3])};
      *(u32x2*)(smem + (wm * 48 + i * 16 + r) * 528 + (wn * 128 + j * 16 + 4 * g) * 2) = pk;
    }
  __syncthreads();
  short* dst = q_ws + (((size_t)blk * NBATCH + b) * HQ + hq) * SQ * DH;
#pragma unroll
  for (int i = 0; i < 12; ++i) {
    int c = tid + i * 256;
    *(u32x4*)(dst + (size_t)c * 8) = *(const u32x4*)(smem + (c >> 5) * 528 + (c & 31) * 16);
  }
}

#define OCLOB "a0","a1","a2","a3","a4","a5","a6","a7","a8","a9","a10","a11","a12","a13","a14","a15","a16","a17","a18","a19","a20","a21","a22","a23","a24","a25","a26","a27","a28","a29","a30","a31","a32","a33","a34","a35","a36","a37","a38","a39","a40","a41","a42","a43","a44","a45","a46","a47","a48","a49","a50","a51","a52","a53","a54","a55","a56","a57","a58","a59","a60","a61","a62","a63","a64","a65","a66","a67","a68","a69","a70","a71","a72","a73","a74","a75","a76","a77","a78","a79","a80","a81","a82","a83","a84","a85","a86","a87","a88","a89","a90","a91","a92","a93","a94","a95","a96","a97","a98","a99","a100","a101","a102","a103","a104","a105","a106","a107","a108","a109","a110","a111","a112","a113","a114","a115","a116","a117","a118","a119","a120","a121","a122","a123","a124","a125","a126","a127","a128","a129","a130","a131","a132","a133","a134","a135","a136","a137","a138","a139","a140","a141","a142","a143","a144","a145","a146","a147","a148","a149","a150","a151","a152","a153","a154","a155","a156","a157","a158","a159","a160","a161","a162","a163","a164","a165","a166","a167","a168","a169","a170","a171","a172","a173","a174","a175","a176","a177","a178","a179","a180","a181","a182","a183","a184","a185","a186","a187","a188","a189","a190","a191"
template <int IDX> __device__ __forceinline__ void o_mfma(bf16x8 vf, bf16x8 pf) {
  asm volatile("v_mfma_f32_16x16x32_bf16 a[%c2:%c3], %0, %1, a[%c2:%c3]" :: "v"(vf), "v"(pf), "i"(IDX * 4), "i"(IDX * 4 + 3) : OCLOB);
}
template <int IDX> __device__ __forceinline__ void o_zero() {
  asm volatile("v_accvgpr_write_b32 a[%c0], 0\n\tv_accvgpr_write_b32 a[%c1], 0\n\tv_accvgpr_write_b32 a[%c2], 0\n\tv_accvgpr_write_b32 a[%c3], 0"
               :: "i"(IDX * 4), "i"(IDX * 4 + 1), "i"(IDX * 4 + 2), "i"(IDX * 4 + 3) : OCLOB);
}
template <int IDX> __device__ __forceinline__ void o_scale(float al) {
  float t0, t1, t2, t3;
  asm volatile("v_accvgpr_read_b32 %0, a[%c5]\n\tv_accvgpr_read_b32 %1, a[%c6]\n\tv_accvgpr_read_b32 %2, a[%c7]\n\tv_accvgpr_read_b32 %3, a[%c8]\n\t"
               "v_mul_f32 %0, %0, %4\n\tv_mul_f32 %1, %1, %4\n\tv_mul_f32 %2, %2, %4\n\tv_mul_f32 %3, %3, %4\n\t"
               "v_accvgpr_write_b32 a[%c5], %0\n\tv_accvgpr_write_b32 a[%c6], %1\n\tv_accvgpr_write_b32 a[%c7], %2\n\tv_accvgpr_write_b32 a[%c8], %3"
               : "=&v"(t0), "=&v"(t1), "=&v"(t2), "=&v"(t3)
               : "v"(al), "i"(IDX * 4), "i"(IDX * 4 + 1), "i"(IDX * 4 + 2), "i"(IDX * 4 + 3) : OCLOB);
}
template <int IDX> __device__ __forceinline__ f32x4 o_read() {
  float t0, t1, t2, t3;
  asm volatile("v_accvgpr_read_b32 %0, a[%c4]\n\tv_accvgpr_read_b32 %1, a[%c5]\n\tv_accvgpr_read_b32 %2, a[%c6]\n\tv_accvgpr_read_b32 %3, a[%c7]"
               : "=v"(t0), "=v"(t1), "=v"(t2), "=v"(t3)
               : "i"(IDX * 4), "i"(IDX * 4 + 1), "i"(IDX * 4 + 2), "i"(IDX * 4 + 3) : OCLOB);
  return f32x4{t0, t1, t2, t3};
}
__device__ __forceinline__ void s_mfma0(f32x4& d, bf16x8 a, bf16x8 b) {
  asm volatile("v_mfma_f32_16x16x32_bf16 %0, %1, %2, 0" : "=&v"(d) : "v"(a), "v"(b));
}
__device__ __forceinline__ void s_mfma(f32x4& d, bf16x8 a, bf16x8 b) {
  asm volatile("v_mfma_f32_16x16x32_bf16 %0, %1, %2, %0" : "+v"(d) : "v"(a), "v"(b));
}
__device__ __forceinline__ void dma16(const void* gsrc, unsigned lds_dst) {
  unsigned keep;
  asm volatile("s_mov_b32 %0, m0\n\ts_mov_b32 m0, %2\n\ts_nop 0\n\tglobal_load_lds_dwordx4 %1, off\n\ts_mov_b32 m0, %0"
               : "=&s"(keep) : "v"(gsrc), "s"(lds_dst) : "memory");
}
template <int N, typename F> __device__ __forceinline__ void static_for(F&& f) {
  if constexpr (N > 0) { static_for<N - 1>(f); f(std::integral_constant<int, N - 1>{}); }
}

constexpr int KT = 32;
constexpr int HALF_B = KT * DH * 2;
constexpr int STAGE_B = 2 * HALF_B;
constexpr int NSTAGE = 4;
constexpr int ATT_LDS = NSTAGE * STAGE_B;
constexpr int KSPLIT = 2;
constexpr int NTILE = SK / KSPLIT / KT;

__global__ __launch_bounds__(256, 1) void attn_kernel(const short* __restrict__ q_ws,
                                                      const short* __restrict__ K0, const short* __restrict__ V0,
                                                      const short* __restrict__ K1, const short* __restrict__ V1,
                                                      short* __restrict__ po, float* __restrict__ pml) {
  extern __shared__ __attribute__((aligned(16))) char smem[];
  const int tid = threadIdx.x, lane = tid & 63;
  const int w = __builtin_amdgcn_readfirstlane(tid >> 6);
  const int r = lane & 15, g = lane >> 4;
  const int id = blockIdx.x;
  const int ks = (id >> 3) & 1, hkv = id & 7, b = (id >> 4) & 7, blk = id >> 7;
  const int hq = hkv * 2 + (w >> 1), rh = w & 1;
  const short* Kg = (blk ? K1 : K0) + (((size_t)b * HKV + hkv) * SK + ks * (SK / KSPLIT)) * DH;
  const short* Vg = (blk ? V1 : V0) + (((size_t)b * HKV + hkv) * SK + ks * (SK / KSPLIT)) * DH;
  const short* Qg = q_ws + (((size_t)blk * NBATCH + b) * HQ + hq) * SQ * DH;

  auto stage = [&](int t) {
    char* kd = smem + (t & (NSTAGE - 1)) * STAGE_B;
    char* vd = kd + HALF_B;
    int ln = lane;
    asm volatile("" : "+v"(ln));
    const int hh = ln >> 5, cp = ln & 31;
    const short* kbase = Kg + (size_t)(t * KT + w * 8 + hh) * DH;
    const short* vbase = Vg + (size_t)(t * KT + w * 8 + hh) * DH;
#pragma unroll
    for (int i = 0; i < 4; ++i) {
      int inst = w * 4 + i;
      int k15 = (w * 8 + 2 * i + hh) & 15;
      const short* ksrc = kbase + i * 2 * DH + ((cp ^ k15) * 8);
      dma16(ksrc, __builtin_amdgcn_readfirstlane((unsigned)(uintptr_t)(kd + inst * 1024)));
      const short* vsrc = vbase + i * 2 * DH + ((cp ^ ((k15 & 7) << 1)) * 8);
      dma16(vsrc, __builtin_amdgcn_readfirstlane((unsigned)(uintptr_t)(vd + inst * 1024)));
    }
  };
  bf16x8 qf[3][8];
#pragma unroll
  for (int rt = 0; rt < 3; ++rt)
#pragma unroll
    for (int s = 0; s < 8; ++s)
      asm volatile("global_load_dwordx4 %0, %1, off" : "=v"(qf[rt][s])
                   : "v"(Qg + (size_t)(rh * 48 + rt * 16 + r) * DH + s * 32 + g * 8) : "memory");
  stage(0); stage(1); stage(2);
  asm volatile("s_waitcnt vmcnt(16)" : "+v"(qf[0][0]), "+v"(qf[0][1]), "+v"(qf[0][2]), "+v"(qf[0][3]), "+v"(qf[0][4]), "+v"(qf[0][5]), "+v"(qf[0][6]), "+v"(qf[0][7]),
               "+v"(qf[1][0]), "+v"(qf[1][1]), "+v"(qf[1][2]), "+v"(qf[1][3]), "+v"(qf[1][4]), "+v"(qf[1][5]), "+v"(qf[1][6]), "+v"(qf[1][7]),
               "+v"(qf[2][0]), "+v"(qf[2][1]), "+v"(qf[2][2]), "+v"(qf[2][3]), "+v"(qf[2][4]), "+v"(qf[2][5]), "+v"(qf[2][6]), "+v"(qf[2][7]) :: "memory");

  static_for<48>([&](auto I) { o_zero<I.value>(); });
  float mrow[3] = {-INFINITY, -INFINITY, -INFINITY};
  float lrow[3] = {0.f, 0.f, 0.f};

  int kaddr[4], vaddr[8];
#pragma unroll
  for (int c = 0; c < 4; ++c) kaddr[c] = r * 512 + (((4 * c + g) ^ r) * 16);
  {
    int q = r >> 2, p4 = r & 3;
    int key0 = 4 * g + q;
#pragma unroll
    for (int c = 0; c < 8; ++c)
      vaddr[c] = HALF_B + key0 * 512 + (((2 * c + (p4 >> 1)) ^ ((key0 & 7) << 1)) * 16) + 8 * (p4 & 1);
  }


#define SB0 __builtin_amdgcn_sched_barrier(0)
  f32x4 st[3][2], sn[3][2];
  __builtin_amdgcn_s_barrier();
#pragma unroll
  for (int sh = 0; sh < 2; ++sh) {
    bf16x8 kfr[4][2];
#pragma unroll
    for (int s4 = 0; s4 < 4; ++s4)
#pragma unroll
      for (int kt = 0; kt < 2; ++kt)
        kfr[s4][kt] = *(const bf16x8*)(smem + kaddr[s4] + sh * 256 + kt * 8192);
#pragma unroll
    for (int s4 = 0; s4 < 4; ++s4)
#pragma unroll
      for (int kt = 0; kt < 2; ++kt)
#pragma unroll
        for (int rt = 0; rt < 3; ++rt) {
          if (sh == 0 && s4 == 0) s_mfma0(st[rt][kt], kfr[s4][kt], qf[rt][sh * 4 + s4]);
          else s_mfma(st[rt][kt], kfr[s4][kt], qf[rt][sh * 4 + s4]);
        }
  }
  asm volatile("s_nop 7\n\ts_nop 7" : "+v"(st[0][0]), "+v"(st[0][1]), "+v"(st[1][0]), "+v"(st[1][1]), "+v"(st[2][0]), "+v"(st[2][1]));

#pragma unroll 1
  for (int t = 0; t < NTILE; ++t) {
    const int sb = (t & (NSTAGE - 1)) * STAGE_B;
    const int sbn = ((t + 1) & (NSTAGE - 1)) * STAGE_B;
    if (t < NTILE - 2) asm volatile("s_waitcnt vmcnt(8) lgkmcnt(0)" ::: "memory");
    else asm volatile("s_waitcnt vmcnt(0) lgkmcnt(0)" ::: "memory");
    __builtin_amdgcn_s_barrier();
    if (t + 3 < NTILE) stage(t + 3);
    bf16x8 ka[4][2], kb[4][2];
#pragma unroll
    for (int s4 = 0; s4 < 4; ++s4)
#pragma unroll
      for (int kt = 0; kt < 2; ++kt)
        ka[s4][kt] = *(const bf16x8*)(smem + kaddr[s4] + sbn + kt * 8192);
#pragma unroll
    for (int s4 = 0; s4 < 4; ++s4)
#pragma unroll
      for (int kt = 0; kt < 2; ++kt)
        kb[s4][kt] = *(const bf16x8*)(smem + kaddr[s4] + sbn + 256 + kt * 8192);
    bf16x8 pf[3];
    float al[3], mx[3], p[3][8];
    auto qk = [&](auto I) {
      constexpr int i = I.value, s = i >> 1, kt = i & 1;
      SB0;
#pragma unroll
      for (int rt = 0; rt < 3; ++rt) {
        if (s == 0) s_mfma0(sn[rt][kt], ka[0][kt], qf[rt][0]);
        else if (s < 4) s_mfma(sn[rt][kt], ka[s & 3][kt], qf[rt][s]);
        else s_mfma(sn[rt][kt], kb[s & 3][kt], qf[rt][s]);
      }
      SB0;
    };
    auto c_max = [&](int rt) {
      float v = fmaxf(fmaxf(fmaxf(st[rt][0][0], st[rt][0][1]), fmaxf(st[rt][0][2], st[rt][0][3])),
                      fmaxf(fmaxf(st[rt][1][0], st[rt][1][1]), fmaxf(st[rt][1][2], st[rt][1][3])));
      auto r16 = __builtin_amdgcn_permlane16_swap(__float_as_uint(v), __float_as_uint(v), false, false);
      v = fmaxf(__uint_as_float(r16[0]), __uint_as_float(r16[1]));
      auto r32 = __builtin_amdgcn_permlane32_swap(__float_as_uint(v), __float_as_uint(v), false, false);
      mx[rt] = fmaxf(__uint_as_float(r32[0]), __uint_as_float(r32[1])) * SCALE_LOG2E;
    };
    auto c_m = [&](int rt) {
      float mnew = (mx[rt] - mrow[rt] > 8.f) ? mx[rt] : mrow[rt];
      al[rt] = __builtin_amdgcn_exp2f(mrow[rt] - mnew);
      mrow[rt] = mnew;
    };
    auto c_exp = [&](int rt, int kt) {
#pragma unroll
      for (int i = 0; i < 4; ++i) p[rt][kt * 4 + i] = __builtin_amdgcn_exp2f(fmaf(st[rt][kt][i], SCALE_LOG2E, -mrow[rt]));
    };
    auto c_fin = [&](int rt) {
      float ps = ((p[rt][0] + p[rt][1]) + (p[rt][2] + p[rt][3])) + ((p[rt][4] + p[rt][5]) + (p[rt][6] + p[rt][7]));
      lrow[rt] = lrow[rt] * al[rt] + ps;
      u32x4 pw = {pack_bf16(p[rt][0], p[rt][1]), pack_bf16(p[rt][2], p[rt][3]), pack_bf16(p[rt][4], p[rt][5]), pack_bf16(p[rt][6], p[rt][7])};
      pf[rt] = __builtin_bit_cast(bf16x8, pw);
    };
#define QK(i) qk(std::integral_constant<int, i>{})
    c_max(0); QK(0); c_max(1); QK(1); c_max(2); QK(2);
    c_m(0); c_m(1); c_m(2); QK(3);
    c_exp(0, 0); QK(4); c_exp(0, 1); QK(5); c_fin(0); QK(6);
    c_exp(1, 0); QK(7); c_exp(1, 1); QK(8); c_fin(1); QK(9);
    c_exp(2, 0); QK(10); c_exp(2, 1); QK(11); c_fin(2); QK(12);
    bf16x8 vfr[8];
#pragma unroll
    for (int dt = 0; dt < 8; ++dt) {
      const char* va = smem + vaddr[dt] + sb;
      s16x4 lo = tr_read(va);
      s16x4 hi = tr_read(va + 8192);
      vfr[dt] = bf16x8{lo[0], lo[1], lo[2], lo[3], hi[0], hi[1], hi[2], hi[3]};
    }
    QK(13); QK(14); QK(15);
#undef QK
    if (__any((al[0] != 1.f) | (al[1] != 1.f) | (al[2] != 1.f))) {
      static_for<48>([&](auto I) { o_scale<I.value>(al[I.value / 16]); });
    }
    asm volatile("s_nop 4" : "+v"(pf[0]), "+v"(pf[1]), "+v"(pf[2]));
    bf16x8 vfr2[8];
#pragma unroll
    for (int dt = 0; dt < 8; ++dt) {
      const char* va = smem + vaddr[dt] + sb + 256;
      s16x4 lo = tr_read(va);
      s16x4 hi = tr_read(va + 8192);
      vfr2[dt] = bf16x8{lo[0], lo[1], lo[2], lo[3], hi[0], hi[1], hi[2], hi[3]};
    }
    static_for<8>([&](auto D) {
      constexpr int dt = D.value;
      o_mfma<dt>(vfr[dt], pf[0]);
      o_mfma<16 + dt>(vfr[dt], pf[1]);
      o_mfma<32 + dt>(vfr[dt], pf[2]);
    });
    static_for<8>([&](auto D) {
      constexpr int dt = D.value;
      o_mfma<8 + dt>(vfr2[dt], pf[0]);
      o_mfma<24 + dt>(vfr2[dt], pf[1]);
      o_mfma<40 + dt>(vfr2[dt], pf[2]);
    });
    asm volatile("s_nop 1" : "+v"(sn[0][0]), "+v"(sn[0][1]), "+v"(sn[1][0]), "+v"(sn[1][1]), "+v"(sn[2][0]), "+v"(sn[2][1]));
#pragma unroll
    for (int rt = 0; rt < 3; ++rt) { st[rt][0] = sn[rt][0]; st[rt][1] = sn[rt][1]; }
  }
  float inv[3];
#pragma unroll
  for (int rt = 0; rt < 3; ++rt) {
    lrow[rt] += __shfl_xor(lrow[rt], 16);
    lrow[rt] += __shfl_xor(lrow[rt], 32);
    inv[rt] = 1.0f / lrow[rt];
    if (g == 0) {
      int row = b * SQ + rh * 48 + rt * 16 + r;
      float* ml = pml + ((((size_t)ks * 2 + blk) * MROWS + row) * HQ + hq) * 2;
      *(f32x2*)ml = f32x2{mrow[rt], lrow[rt]};
    }
  }
  asm volatile("s_nop 15\n\ts_nop 15");
  __syncthreads();
  constexpr int ORS = 528;
  char* ost = smem + w * (48 * ORS);
  static_for<48>([&](auto I) {
    constexpr int rt = I.value / 16, dt = I.value % 16;
    f32x4 v = o_read<I.value>();
    u32x2 pk = {pack_bf16(v[0] * inv[rt], v[1] * inv[rt]), pack_bf16(v[2] * inv[rt], v[3] * inv[rt])};
    *(u32x2*)(ost + (rt * 16 + r) * ORS + dt * 32 + g * 8) = pk;
  });
  asm volatile("s_waitcnt lgkmcnt(0)" ::: "memory");
  {
    short* dbase = po + (((size_t)ks * 2 + blk) * MROWS + b * SQ + rh * 48) * HD + hq * DH;
#pragma unroll
    for (int i = 0; i < 24; ++i) {
      int row = i * 2 + (lane >> 5), c = lane & 31;
      u32x4 v = *(const u32x4*)(ost + row * ORS + c * 16);
      *(u32x4*)(dbase + (size_t)row * HD + c * 8) = v;
    }
  }
}

constexpr int OB_STRIDE = 464;
constexpr int OA_BYTES = 96 * 128, OB_BYTES = 64 * OB_STRIDE, OSTAGE = OA_BYTES + OB_BYTES;
__global__ __launch_bounds__(256, 1) void oproj_kernel(const short* __restrict__ po, const float* __restrict__ pml,
                                                       const short* __restrict__ W0, const short* __restrict__ W1,
                                                       float* __restrict__ part) {
  extern __shared__ __attribute__((aligned(16))) char smem[];
  const int tid = threadIdx.x, lane = tid & 63, w = tid >> 6;
  const int r = lane & 15, g = lane >> 4;
  const int bz = blockIdx.x & 7, rest = blockIdx.x >> 3;
  const int blk = rest >> 4, tm = (rest >> 1) & 7, tn = rest & 1;
  const int m0 = tm * 96, n0 = tn * 224;
  const int wm = w & 1, wn = w >> 1;
  const short* A = po + ((size_t)blk * MROWS + m0) * HD + bz * 512;
  const short* A2 = A + (size_t)2 * MROWS * HD;
  const short* B = (blk ? W1 : W0) + (size_t)bz * 512 * DM + n0;

  float w0[2][3], w1[2][3];
#pragma unroll
  for (int h = 0; h < 2; ++h)
#pragma unroll
    for (int i = 0; i < 3; ++i) {
      size_t rowh = ((size_t)blk * MROWS + m0 + ((tid + i * 256) >> 3)) * HQ + bz * 2 + h;
      f32x2 a = *(const f32x2*)(pml + rowh * 2);
      f32x2 c = *(const f32x2*)(pml + ((size_t)2 * MROWS * HQ + rowh) * 2);
      float mm = fmaxf(a[0], c[0]);
      float x0 = a[1] * __builtin_amdgcn_exp2f(a[0] - mm), x1 = c[1] * __builtin_amdgcn_exp2f(c[0] - mm);
      float iv = 1.0f / (x0 + x1);
      w0[h][i] = x0 * iv; w1[h][i] = x1 * iv;
    }

  f32x4 acc[3][7];
#pragma unroll
  for (int i = 0; i < 3; ++i)
#pragma unroll
    for (int j = 0; j < 7; ++j) acc[i][j] = f32x4{0.f, 0.f, 0.f, 0.f};

  bf16x8 ra[2][3], ra2[2][3], rb[2][7];
  auto gload = [&](int kt, int set) {
#pragma unroll
    for (int i = 0; i < 3; ++i) {
      int c = tid + i * 256;
      ra[set][i] = *(const bf16x8*)(A + (size_t)(c >> 3) * HD + kt * 64 + (c & 7) * 8);
      ra2[set][i] = *(const bf16x8*)(A2 + (size_t)(c >> 3) * HD + kt * 64 + (c & 7) * 8);
    }
#pragma unroll
    for (int i = 0; i < 7; ++i) {
      int c = tid + i * 256;
      rb[set][i] = *(const bf16x8*)(B + (size_t)(kt * 64 + c / 28) * DM + (c % 28) * 8);
    }
  };
  gload(0, 0);
  gload(1, 1);
#pragma unroll
  for (int kt = 0; kt < 8; ++kt) {
    const int set = kt & 1, h = kt >> 2;
    char* sa = smem + set * OSTAGE;
    char* sb = sa + OA_BYTES;
#pragma unroll
    for (int i = 0; i < 3; ++i) {
      int c = tid + i * 256;
      int row = c >> 3, ch = c & 7;
      u32x4 x = __builtin_bit_cast(u32x4, ra[set][i]), y = __builtin_bit_cast(u32x4, ra2[set][i]), z;
#pragma unroll
      for (int e = 0; e < 4; ++e) {
        float x0 = __uint_as_float(x[e] << 16), x1 = __uint_as_float(x[e] & 0xffff0000u);
        float y0 = __uint_as_float(y[e] << 16), y1 = __uint_as_float(y[e] & 0xffff0000u);
        z[e] = pack_bf16(x0 * w0[h][i] + y0 * w1[h][i], x1 * w0[h][i] + y1 * w1[h][i]);
      }
      *(u32x4*)(sa + row * 128 + ((ch ^ (row & 7)) * 16)) = z;
    }
#pragma unroll
    for (int i = 0; i < 7; ++i) {
      int c = tid + i * 256;
      *(bf16x8*)(sb + (c / 28) * OB_STRIDE + (c % 28) * 16) = rb[set][i];
    }
    __builtin_amdgcn_sched_barrier(0);
    if (kt + 2 < 8) gload(kt + 2, set);
    __builtin_amdgcn_sched_barrier(0);
    __syncthreads();
#pragma unroll
    for (int ks = 0; ks < 2; ++ks) {
      bf16x8 af[3], bfr[7];
#pragma unroll
      for (int i = 0; i < 3; ++i) {
        int row = wm * 48 + i * 16 + r;
        af[i] = *(const bf16x8*)(sa + row * 128 + (((ks * 4 + g) ^ (row & 7)) * 16));
      }
      const char* bb = sb + (ks * 32 + 8 * g + (r >> 2)) * OB_STRIDE + (wn * 112 + (r & 3) * 4) * 2;
#pragma unroll
      for (int j = 0; j < 7; ++j) {
        s16x4 lo = tr_read(bb + j * 32);
        s16x4 hi = tr_read(bb + j * 32 + 4 * OB_STRIDE);
        bfr[j] = bf16x8{lo[0], lo[1], lo[2], lo[3], hi[0], hi[1], hi[2], hi[3]};
      }
#pragma unroll
      for (int i = 0; i < 3; ++i)
#pragma unroll
        for (int j = 0; j < 7; ++j)
          acc[i][j] = __builtin_amdgcn_mfma_f32_16x16x32_bf16(bfr[j], af[i], acc[i][j], 0, 0, 0);
    }
  }
#pragma unroll
  for (int i = 0; i < 3; ++i)
#pragma unroll
    for (int j = 0; j < 7; ++j) {
      int m = m0 + wm * 48 + i * 16 + r;
      int n = n0 + wn * 112 + j * 16 + 4 * g;
      *(f32x4*)(part + (((size_t)bz * 2 + blk) * MROWS + m) * DM + n) = acc[i][j];
    }
}

constexpr int OSPLIT = 8;
__global__ __launch_bounds__(256) void reduce_kernel(const float* __restrict__ part, short* __restrict__ out) {
  int i = (blockIdx.x * 256 + threadIdx.x) * 4;
  f32x4 a = *(const f32x4*)(part + i);
#pragma unroll
  for (int z = 1; z < OSPLIT; ++z) a += *(const f32x4*)(part + (size_t)z * 2 * MROWS * DM + i);
  int blk = i / (MROWS * DM), rem = i % (MROWS * DM), m = rem / DM, n = rem % DM;
  u32x2 pk = {pack_bf16(a[0], a[1]), pack_bf16(a[2], a[3])};
  *(u32x2*)(out + (size_t)m * 2 * DM + blk * DM + n) = pk;
}

extern "C" void kernel_launch(void* const* d_in, const int* in_sizes, int n_in,
                              void* d_out, int out_size, void* d_ws, size_t ws_size,
                              hipStream_t stream) {
  const short* Ax = (const short*)d_in[0];
  const short* Ak = (const short*)d_in[1];
  const short* Av = (const short*)d_in[2];
  const short* Awq = (const short*)d_in[3];
  const short* Awo = (const short*)d_in[4];
  const short* Bx = (const short*)d_in[5];
  const short* Bk = (const short*)d_in[6];
  const short* Bv = (const short*)d_in[7];
  const short* Bwq = (const short*)d_in[8];
  const short* Bwo = (const short*)d_in[9];
  short* out = (short*)d_out;
  const size_t n = (size_t)MROWS * HD;
  short* q_ws = (short*)d_ws;
  short* po = q_ws + 2 * n;
  float* pml = (float*)(po + 4 * n);

  (void)hipFuncSetAttribute((const void*)attn_kernel, hipFuncAttributeMaxDynamicSharedMemorySize, ATT_LDS);

  (void)hipFuncSetAttribute((const void*)qproj_kernel, hipFuncAttributeMaxDynamicSharedMemorySize, 2 * QSTAGE);
  qproj_kernel<<<256, 256, 2 * QSTAGE, stream>>>(Ax, Bx, Awq, Bwq, q_ws);
  attn_kernel<<<256, 256, ATT_LDS, stream>>>(q_ws, Ak, Av, Bk, Bv, po, pml);
  float* part = pml + 1024 * 1024;
  (void)hipFuncSetAttribute((const void*)oproj_kernel, hipFuncAttributeMaxDynamicSharedMemorySize, 2 * OSTAGE);
  oproj_kernel<<<256, 256, 2 * OSTAGE, stream>>>(po, pml, Awo, Bwo, part);
  reduce_kernel<<<2 * MROWS * DM / 4 / 256, 256, 0, stream>>>(part, out);
}
```
